```python
import torch

def _softmax(x, axis=-1):
    return torch.softmax(x, dim=axis)
def _gelu(x):
    return torch.nn.functional.gelu(x, approximate="tanh")
def _silu(x):
    return torch.nn.functional.silu(x)
def _sigmoid(x):
    return torch.sigmoid(x)
def _layernorm(x, axis=-1, eps=1e-5):
    m = x.mean(dim=axis, keepdim=True)
    v = x.var(dim=axis, keepdim=True, unbiased=False)
    return (x - m) / torch.sqrt(v + eps)
def _scatter_add(tgt, idx, src, axis=0):
    return tgt.clone().index_add_(axis, idx, src)
def _segment_sum(src, seg, n):
    out = torch.zeros((n,) + src.shape[1:], dtype=src.dtype)
    return out.index_add_(0, seg, src)
def _pad(x, pw):
    flat = tuple(v for pair in reversed(pw) for v in pair)
    return torch.nn.functional.pad(x, flat)

kernel_name = 'concat_heads__gqa_block_cached__gqa_block_cached__near_ridge'

def setup_inputs():

    _w = (lambda a: (lambda t: t.to(torch.bfloat16) if t.is_floating_point() else t)(torch.as_tensor(a)))
    def _A_setup():
        B, Sq, D = 8, 96, 640
        Hq, Hkv, Sk, Dh = 32, 32, 512, 128
        assert Hq % Hkv == 0, (Hq, Hkv)
        return {
            "x": _w((lambda *s: torch.randn(*s, dtype=torch.bfloat16) * 0.8)(B, Sq, D)),
            "k_cache": _w((lambda *s: torch.randn(*s, dtype=torch.bfloat16) * 0.8)(B, Hkv, Sk, Dh)),
            "v_cache": _w((lambda *s: torch.randn(*s, dtype=torch.bfloat16) * 0.05)(B, Hkv, Sk, Dh)),
            "wq": _w((lambda *s: torch.randn(*s, dtype=torch.bfloat16) * 0.8)(D, Hq * Dh) * (D ** -0.5)),
            "wo": _w((lambda *s: torch.randn(*s, dtype=torch.bfloat16) * 0.05)(Hq * Dh, D) * ((Hq * Dh) ** -0.5)),
        }
    _A_in = _A_setup()

    def _B_setup():
        B, Sq, D = 8, 96, 640
        Hq, Hkv, Sk, Dh = 32, 32, 512, 128
        assert Hq % Hkv == 0, (Hq, Hkv)
        return {
            "x": _w((lambda *s: torch.randn(*s, dtype=torch.bfloat16) * 0.8)(B, Sq, D)),
            "k_cache": _w((lambda *s: torch.randn(*s, dtype=torch.bfloat16) * 0.8)(B, Hkv, Sk, Dh)),
            "v_cache": _w((lambda *s: torch.randn(*s, dtype=torch.bfloat16) * 0.05)(B, Hkv, Sk, Dh)),
            "wq": _w((lambda *s: torch.randn(*s, dtype=torch.bfloat16) * 0.8)(D, Hq * Dh) * (D ** -0.5)),
            "wo": _w((lambda *s: torch.randn(*s, dtype=torch.bfloat16) * 0.05)(Hq * Dh, D) * ((Hq * Dh) ** -0.5)),
        }
    _B_in = _B_setup()

    return {
        "A_x": _A_in["x"],
        "A_k_cache": _A_in["k_cache"],
        "A_v_cache": _A_in["v_cache"],
        "A_wq": _A_in["wq"],
        "A_wo": _A_in["wo"],
        "B_x": _B_in["x"],
        "B_k_cache": _B_in["k_cache"],
        "B_v_cache": _B_in["v_cache"],
        "B_wq": _B_in["wq"],
        "B_wo": _B_in["wo"],
    }

def reference(A_x, A_k_cache, A_v_cache, A_wq, A_wo, B_x, B_k_cache, B_v_cache, B_wq, B_wo):
    def _A_ref(x, k_cache, v_cache, wq, wo):
        B, Sq, D = x.shape
        Hq, Dh = 32, 128
        Hkv = k_cache.shape[1]
        rep = Hq // Hkv
        q = (lambda x, *d: x.permute(*d))((x @ wq).reshape(B, Sq, Hq, Dh), 0, 2, 1, 3)
        k = (lambda x, n, axis: torch.repeat_interleave(x, n, dim=axis))(k_cache, rep, axis=1)
        v = (lambda x, n, axis: torch.repeat_interleave(x, n, dim=axis))(v_cache, rep, axis=1)
        scores = torch.einsum("bhqd,bhkd->bhqk", q, k) * (Dh ** -0.5)
        p = _softmax(scores, axis=-1)
        o = torch.einsum("bhqk,bhkd->bhqd", p, v)
        return (lambda x, *d: x.permute(*d))(o, 0, 2, 1, 3).reshape(B, Sq, Hq * Dh) @ wo

    def _B_ref(x, k_cache, v_cache, wq, wo):
        B, Sq, D = x.shape
        Hq, Dh = 32, 128
        Hkv = k_cache.shape[1]
        rep = Hq // Hkv
        q = (lambda x, *d: x.permute(*d))((x @ wq).reshape(B, Sq, Hq, Dh), 0, 2, 1, 3)
        k = (lambda x, n, axis: torch.repeat_interleave(x, n, dim=axis))(k_cache, rep, axis=1)
        v = (lambda x, n, axis: torch.repeat_interleave(x, n, dim=axis))(v_cache, rep, axis=1)
        scores = torch.einsum("bhqd,bhkd->bhqk", q, k) * (Dh ** -0.5)
        p = _softmax(scores, axis=-1)
        o = torch.einsum("bhqk,bhkd->bhqd", p, v)
        return (lambda x, *d: x.permute(*d))(o, 0, 2, 1, 3).reshape(B, Sq, Hq * Dh) @ wo

    a = _A_ref(A_x, A_k_cache, A_v_cache, A_wq, A_wo)
    b = _B_ref(B_x, B_k_cache, B_v_cache, B_wq, B_wo)
    return torch.concatenate([a, b], axis=-1)
```

```cpp
#include <hip/hip_runtime.h>
#include <hip/hip_bf16.h>

typedef __bf16 bf16x8 __attribute__((ext_vector_type(8)));
typedef __bf16 bf16x2 __attribute__((ext_vector_type(2)));
typedef float f32x2 __attribute__((ext_vector_type(2)));
typedef float f32x4 __attribute__((ext_vector_type(4)));
typedef unsigned u32x4 __attribute__((ext_vector_type(4)));
typedef unsigned u32x8 __attribute__((ext_vector_type(8)));
typedef unsigned u32x16 __attribute__((ext_vector_type(16)));
typedef unsigned u32x32 __attribute__((ext_vector_type(32)));
typedef unsigned long long u64;
#define DI __device__ __forceinline__
#define LDSP(T, a) ((__attribute__((address_space(3))) T*)(size_t)(a))
constexpr unsigned sched_mask(const char* p) { return *p ? (sched_mask(p + 1) << 1) | (*p == 'Q') : 1; }
constexpr unsigned ORDER = sched_mask("QQQUUUUUUQQQQQQQU"), ORDER2 = sched_mask("QQQQQQQQQQU");

constexpr int DM = 640, NH = 32, SK = 512, DH = 128;
constexpr int LDS_BYTES = 163840;
constexpr unsigned SLOT = 40960;
constexpr unsigned CTL = 36864;
constexpr unsigned O_BYTES = 2 * 768 * 4096 * 2, FLAG_BYTES = 256 * 128;
constexpr u64 TAG = 0x9E3779B97F4A7C15ull;
constexpr float SCL = 0.08838834764831845f * 1.4426950408889634f;

DI int lane_id() { int l; asm volatile("v_mbcnt_lo_u32_b32 %0, -1, 0\n\tv_mbcnt_hi_u32_b32 %0, -1, %0" : "=v"(l)); return l; }
DI void bar() { asm volatile("s_waitcnt lgkmcnt(0)\n\ts_barrier" ::: "memory"); }
DI unsigned pk2(float a, float b) { f32x2 v = {a, b}; return __builtin_bit_cast(unsigned, __builtin_convertvector(v, bf16x2)); }
DI bf16x8 pk8(f32x4 a, f32x4 b) { u32x4 w = {pk2(a[0], a[1]), pk2(a[2], a[3]), pk2(b[0], b[1]), pk2(b[2], b[3])}; return __builtin_bit_cast(bf16x8, w); }
DI void post(unsigned a, unsigned v) { asm volatile("" ::: "memory"); *LDSP(volatile unsigned char, a) = (unsigned char)v; asm volatile("" ::: "memory"); }
DI unsigned peek(unsigned a) { return *LDSP(volatile unsigned, a); }
DI void await(unsigned a, unsigned seen, unsigned n) {
  const unsigned t = n * 0x01010101u;
  if (__builtin_expect((__builtin_amdgcn_readfirstlane(seen) - t) & 0x80808080u, 0))
    for (int k = 0; k < (1 << 20) && ((__builtin_amdgcn_readfirstlane(peek(a)) - t) & 0x80808080u); ++k) {}
  asm volatile("" ::: "memory");
}
template <int O, class V> DI void srd(V& v, const char* p, unsigned bytes) {
  v[O] = (unsigned)(size_t)p; v[O + 1] = (unsigned)((size_t)p >> 32) & 0xffff; v[O + 2] = bytes; v[O + 3] = 0x00020000;
}

#define LOADER_ASM \
  "s_getpc_b64 s[80:81]\n" \
  ".L0_%=:\n" \
  "s_add_u32 s82, s80, .Lld_%=-.L0_%=\n s_addc_u32 s83, s81, 0\n" \
  "s_add_u32 s80, s80, .Lst_%=-.L0_%=\n s_addc_u32 s81, s81, 0\n" \
  "s_mov_b32 s64, 1\n s_mov_b32 s65, 0\n s_mov_b32 s66, 0\n s_mov_b32 s67, 0\n s_mov_b32 s68, s63\n s_mov_b32 s69, 0\n" \
  "s_mov_b32 s72, 40960\n s_mov_b32 s73, s56\n s_mov_b32 s74, 0\n s_mov_b32 s75, 0x100000\n s_mov_b32 s76, s58\n" \
  "s_branch .Lq_%=\n" \
  ".Lloop_%=:\n" \
  "ds_read_b32 v24, v22 offset:8\n s_waitcnt lgkmcnt(0)\n v_readfirstlane_b32 s70, v24\n" \
  "s_and_b32 s69, s70, 0xff\n s_bfe_u32 s71, s70, 0x80008\n s_min_u32 s69, s69, s71\n" \
  "s_bfe_u32 s71, s70, 0x80010\n s_min_u32 s69, s69, s71\n s_lshr_b32 s71, s70, 24\n s_min_u32 s69, s69, s71\n" \
  ".Ldump_%=:\n" \
  "s_min_u32 s70, s67, 32\n s_cmp_ge_u32 s66, s70\n s_cbranch_scc1 .Lq_%=\n" \
  "s_add_u32 s70, s66, 1\n s_lshr_b32 s70, s70, 1\n s_add_u32 s71, s70, 7\n" \
  "s_cmp_eq_u32 s66, 0\n s_cselect_b32 s71, 0, s71\n s_cmp_lt_u32 s69, s71\n s_cbranch_scc1 .Lq_%=\n" \
  "s_and_b32 s70, s70, 3\n s_mul_i32 s70, s70, 40960\n v_add_u32 v24, s70, v20\n" \
  "s_swappc_b64 s[84:85], s[80:81]\n" \
  "s_add_u32 s66, s66, 1\n v_mov_b32 v25, s66\n ds_write_b8 v21, v25 offset:4\n s_branch .Ldump_%=\n" \
  ".Lq_%=:\n" \
  "s_cmp_le_u32 s68, 1\n s_cbranch_scc1 .Lkv_%=\n" \
  "s_bitcmp0_b32 s68, 0\n s_cbranch_scc1 .Lkv_%=\n" \
  "s_add_u32 s70, s69, 2\n s_cmp_lt_u32 s70, s64\n s_cbranch_scc1 .Lspin_%=\n" \
  ".Lq1_%=:\n" \
  "s_lshr_b32 s68, s68, 1\n v_mov_b32 v25, s64\n s_add_u32 m0, s72, s60\n s_lshl_b32 s70, s73, 19\n" \
  ".irp p, 0, 1, 2, 3, 4, 5, 6, 7, 8, 9\n" \
  ".if \\p < 4\n buffer_load_dwordx4 v[12+\\p], s[40:43], s70 offen offset:1024*\\p lds\n" \
  ".else\n buffer_load_dwordx4 v[2+\\p], s[36:39], s70 offen offset:1024*((\\p-4)%%3) lds\n .endif\n" \
  ".if \\p == 3\n s_add_u32 m0, s72, s61\n s_lshl_b32 s70, s73, 7\n .endif\n" \
  ".if \\p == 6\n s_add_u32 m0, m0, 3072\n s_add_u32 s73, s73, 1\n .endif\n" \
  ".if \\p < 9\n s_sleep 1\n .endif\n" \
  ".if \\p+1 == 6\n s_waitcnt vmcnt(\\p+1)\n ds_write_b8 v21, v25\n .endif\n" \
  ".endr\n" \
  "s_cmp_eq_u32 s73, 10\n s_cselect_b32 s73, 0, s73\n" \
  "s_cmp_eq_u32 s72, 3*40960\n s_cselect_b32 s72, 0, s72\n s_add_u32 s72, s72, 40960\n" \
  "s_mov_b32 s67, s65\n s_add_u32 s64, s64, 1\n s_branch .Lloop_%=\n" \
  ".Lkv_%=:\n" \
  "s_cmp_ge_u32 s65, 32\n s_cbranch_scc1 .Lwo_%=\n" \
  "s_sub_u32 s70, s65, s66\n s_cmp_gt_u32 s70, 10\n s_cbranch_scc1 .Lidle_%=\n" \
  "s_lshr_b32 s68, s68, 1\n" \
  "s_add_u32 s70, s65, %[kr]\n s_and_b32 s70, s70, 31\n s_lshl_b32 s70, s70, 12\n s_waitcnt lgkmcnt(0)\n" \
  "s_swappc_b64 s[84:85], s[82:83]\n" \
  "s_add_u32 s65, s65, 2\n" \
  ".Lpost_%=:\n" \
  "v_mov_b32 v25, s64\n s_waitcnt vmcnt(8)\n ds_write_b8 v21, v25\n" \
  "s_sub_u32 s70, s65, 2\n s_max_i32 s67, s67, s70\n s_branch .Lloop_%=\n" \
  ".Lwo_%=:\n" \
  "s_cmp_ge_u32 s65, s62\n s_cbranch_scc1 .Lfin_%=\n" \
  "s_cmp_lt_u32 s69, s59\n s_cbranch_scc1 .Lidle_%=\n" \
  "s_mov_b32 m0, s76\n s_xor_b32 s70, s74, s57\n s_mul_i32 s70, s70, 40960\n" \
  "buffer_load_dwordx4 v16, s[52:55], s70 offen lds\n buffer_load_dwordx4 v17, s[52:55], s70 offen offset:1024 lds\n" \
  "buffer_load_dwordx4 v18, s[52:55], s70 offen offset:2048 lds\n buffer_load_dwordx4 v19, s[52:55], s70 offen offset:3072 lds\n" \
  "s_add_u32 s76, s76, 4096\n s_add_u32 s74, s74, 1\n s_add_u32 s65, s65, 1\n s_branch .Lpost_%=\n" \
  ".Lfin_%=:\n" \
  "s_cmp_ge_u32 s66, 32\n s_cbranch_scc1 .Lend_%=\n" \
  ".Lidle_%=:\n" \
  "s_add_u32 s70, s67, 2\n s_cmp_eq_u32 s70, s65\n s_cbranch_scc0 .Li0_%=\n" \
  "s_cmp_gt_u32 s68, 1\n s_cbranch_scc1 .Lspin_%=\n" \
  "s_waitcnt vmcnt(4)\n s_add_u32 s67, s67, 1\n s_branch .Lloop_%=\n" \
  ".Li0_%=:\n" \
  "v_mov_b32 v25, s64\n s_waitcnt vmcnt(0)\n ds_write_b8 v21, v25\n" \
  "s_cmp_eq_u32 s67, s65\n s_mov_b32 s67, s65\n s_cbranch_scc0 .Lloop_%=\n" \
  "s_sleep 1\n" \
  ".Lspin_%=:\n" \
  "s_sub_u32 s75, s75, 1\n s_cmp_eq_u32 s75, 0\n s_cbranch_scc0 .Lloop_%=\n" \
  "s_branch .Lend_%=\n" \
  ".Lld_%=:\n" \
  ".set bb, 64\n .rept 6\n" \
  ".set pp, 0\n .rept 4\n buffer_load_dwordx4 v[bb+4*pp:bb+4*pp+3], v0, s[44:47], s70 offen offset:1024*pp nt\n .set pp, pp+1\n .endr\n" \
  ".set pp, 0\n .rept 4\n buffer_load_dwordx4 v[bb+16+4*pp:bb+19+4*pp], v0, s[48:51], s70 offen offset:1024*pp nt\n .set pp, pp+1\n .endr\n" \
  "s_swappc_b64 s[82:83], s[84:85]\n" \
  ".set bb, bb+32\n .endr\n s_branch .Lld_%=\n" \
  ".Lst_%=:\n" \
  ".set bb, 64\n .rept 6\n" \
  "ds_write_b128 v24, v[bb:bb+3]\n ds_write_b128 v24, v[bb+4:bb+7] offset:1152\n" \
  "ds_write_b128 v24, v[bb+8:bb+11] offset:2304\n ds_write_b128 v24, v[bb+12:bb+15] offset:3456\n" \
  "s_swappc_b64 s[80:81], s[84:85]\n" \
  "ds_write_b128 v24, v[bb+16:bb+19] offset:9216\n ds_write_b128 v24, v[bb+20:bb+23] offset:10368\n" \
  "ds_write_b128 v24, v[bb+24:bb+27] offset:11520\n ds_write_b128 v24, v[bb+28:bb+31] offset:12672\n" \
  "s_swappc_b64 s[80:81], s[84:85]\n" \
  ".set bb, bb+32\n .endr\n s_branch .Lst_%=\n" \
  ".Lend_%=:\n"

#define ATT_MACROS \
  ".macro QPRE \n" \
  "ds_read_b64_tr_b16 v[32:33], v8 offset:0\n ds_read_b64_tr_b16 v[34:35], v8 offset:1024\n ds_read_b128 v[64:67], v16 offset:0\n" \
  "ds_read_b128 v[68:71], v16 offset:2048\n ds_read_b128 v[72:75], v16 offset:4096\n ds_read_b64_tr_b16 v[36:37], v9 offset:0\n" \
  "ds_read_b64_tr_b16 v[38:39], v9 offset:1024\n ds_read_b64_tr_b16 v[40:41], v10 offset:0\n ds_read_b64_tr_b16 v[42:43], v10 offset:1024\n" \
  "ds_read_b64_tr_b16 v[44:45], v11 offset:0\n ds_read_b64_tr_b16 v[46:47], v11 offset:1024\n ds_read_b64_tr_b16 v[48:49], v12 offset:0\n" \
  "ds_read_b64_tr_b16 v[50:51], v12 offset:1024\n ds_read_b64_tr_b16 v[52:53], v13 offset:0\n ds_read_b64_tr_b16 v[54:55], v13 offset:1024\n" \
  "s_waitcnt lgkmcnt(8)\n" \
  ".endm\n" \
  ".macro QSTAGE \n" \
  "v_mfma_f32_16x16x32_bf16 v[160:163], v[32:35], v[64:67], v[160:163]\n ds_read_b64_tr_b16 v[56:57], v14 offset:0\n" \
  "ds_read_b64_tr_b16 v[58:59], v14 offset:1024\n" \
  "v_mfma_f32_16x16x32_bf16 v[164:167], v[32:35], v[68:71], v[164:167]\n ds_read_b128 v[76:79], v17 offset:0\n" \
  "v_mfma_f32_16x16x32_bf16 v[168:171], v[32:35], v[72:75], v[168:171]\n s_cmp_eq_u32 s68, 2\n s_cselect_b32 s66, s69, s70\n" \
  "v_mfma_f32_16x16x32_bf16 v[172:175], v[36:39], v[64:67], v[172:175]\n ds_read_b64_tr_b16 v[60:61], v15 offset:0\n" \
  "ds_read_b64_tr_b16 v[62:63], v15 offset:1024\n" \
  "v_mfma_f32_16x16x32_bf16 v[176:179], v[36:39], v[68:71], v[176:179]\n ds_read_b128 v[80:83], v17 offset:2048\n" \
  "v_mfma_f32_16x16x32_bf16 v[180:183], v[36:39], v[72:75], v[180:183]\n s_waitcnt lgkmcnt(10)\n" \
  "v_mfma_f32_16x16x32_bf16 v[184:187], v[40:43], v[64:67], v[184:187]\n ds_read_b64_tr_b16 v[32:33], v8 offset:8192\n" \
  "ds_read_b64_tr_b16 v[34:35], v8 offset:9216\n" \
  "v_mfma_f32_16x16x32_bf16 v[188:191], v[40:43], v[68:71], v[188:191]\n ds_read_b128 v[84:87], v17 offset:4096\n v_add_u32 v8, s66, v8\n" \
  "v_mfma_f32_16x16x32_bf16 v[192:195], v[40:43], v[72:75], v[192:195]\n s_cselect_b32 s68, -1, s68\n s_add_u32 s68, s68, 1\n" \
  "v_mfma_f32_16x16x32_bf16 v[196:199], v[44:47], v[64:67], v[196:199]\n ds_read_b64_tr_b16 v[36:37], v9 offset:8192\n" \
  "ds_read_b64_tr_b16 v[38:39], v9 offset:9216\n" \
  "v_mfma_f32_16x16x32_bf16 v[200:203], v[44:47], v[68:71], v[200:203]\n v_add_u32 v9, s66, v9\n" \
  "v_mfma_f32_16x16x32_bf16 v[204:207], v[44:47], v[72:75], v[204:207]\n v_add_u32 v16, s66, v16\n s_waitcnt lgkmcnt(11)\n" \
  "v_mfma_f32_16x16x32_bf16 v[208:211], v[48:51], v[64:67], v[208:211]\n ds_read_b64_tr_b16 v[40:41], v10 offset:8192\n" \
  "ds_read_b64_tr_b16 v[42:43], v10 offset:9216\n" \
  "v_mfma_f32_16x16x32_bf16 v[212:215], v[48:51], v[68:71], v[212:215]\n v_add_u32 v10, s66, v10\n" \
  "v_mfma_f32_16x16x32_bf16 v[216:219], v[48:51], v[72:75], v[216:219]\n v_add_u32 v17, s66, v17\n" \
  "v_mfma_f32_16x16x32_bf16 v[220:223], v[52:55], v[64:67], v[220:223]\n ds_read_b64_tr_b16 v[44:45], v11 offset:8192\n" \
  "ds_read_b64_tr_b16 v[46:47], v11 offset:9216\n" \
  "v_mfma_f32_16x16x32_bf16 v[224:227], v[52:55], v[68:71], v[224:227]\n v_add_u32 v11, s66, v11\n" \
  "v_mfma_f32_16x16x32_bf16 v[228:231], v[52:55], v[72:75], v[228:231]\n ds_read_u8 v94, v2\n s_waitcnt lgkmcnt(11)\n" \
  "v_mfma_f32_16x16x32_bf16 v[232:235], v[56:59], v[64:67], v[232:235]\n ds_read_b64_tr_b16 v[48:49], v12 offset:8192\n" \
  "ds_read_b64_tr_b16 v[50:51], v12 offset:9216\n" \
  "v_mfma_f32_16x16x32_bf16 v[236:239], v[56:59], v[68:71], v[236:239]\n v_add_u32 v12, s66, v12\n" \
  "v_mfma_f32_16x16x32_bf16 v[240:243], v[56:59], v[72:75], v[240:243]\n" \
  "v_mfma_f32_16x16x32_bf16 v[244:247], v[60:63], v[64:67], v[244:247]\n ds_read_b64_tr_b16 v[52:53], v13 offset:8192\n" \
  "ds_read_b64_tr_b16 v[54:55], v13 offset:9216\n" \
  "v_mfma_f32_16x16x32_bf16 v[248:251], v[60:63], v[68:71], v[248:251]\n v_add_u32 v13, s66, v13\n" \
  "v_mfma_f32_16x16x32_bf16 v[252:255], v[60:63], v[72:75], v[252:255]\n v_mov_b32 v89, s65\n s_waitcnt lgkmcnt(9)\n" \
  "v_mfma_f32_16x16x32_bf16 v[160:163], v[32:35], v[76:79], v[160:163]\n ds_read_b64_tr_b16 v[56:57], v14 offset:8192\n" \
  "ds_read_b64_tr_b16 v[58:59], v14 offset:9216\n" \
  "v_mfma_f32_16x16x32_bf16 v[164:167], v[32:35], v[80:83], v[164:167]\n v_add_u32 v14, s66, v14\n" \
  "v_mfma_f32_16x16x32_bf16 v[168:171], v[32:35], v[84:87], v[168:171]\n" \
  "v_mfma_f32_16x16x32_bf16 v[172:175], v[36:39], v[76:79], v[172:175]\n ds_read_b64_tr_b16 v[60:61], v15 offset:8192\n" \
  "ds_read_b64_tr_b16 v[62:63], v15 offset:9216\n" \
  "v_mfma_f32_16x16x32_bf16 v[176:179], v[36:39], v[80:83], v[176:179]\n v_add_u32 v15, s66, v15\n ds_write_b8 v3, v89\n" \
  "v_mfma_f32_16x16x32_bf16 v[180:183], v[36:39], v[84:87], v[180:183]\n s_waitcnt lgkmcnt(9)\n v_cmp_gt_u32 vcc, s64, v94\n" \
  "s_cbranch_vccnz .Lqw_%=\n .Lqr_%=:\n" \
  "v_mfma_f32_16x16x32_bf16 v[184:187], v[40:43], v[76:79], v[184:187]\n ds_read_b64_tr_b16 v[32:33], v8 offset:0\n" \
  "ds_read_b64_tr_b16 v[34:35], v8 offset:1024\n" \
  "v_mfma_f32_16x16x32_bf16 v[188:191], v[40:43], v[80:83], v[188:191]\n ds_read_b128 v[64:67], v16 offset:0\n ds_read_b128 v[68:71], v16 offset:2048\n" \
  "v_mfma_f32_16x16x32_bf16 v[192:195], v[40:43], v[84:87], v[192:195]\n ds_read_b128 v[72:75], v16 offset:4096\n" \
  "v_mfma_f32_16x16x32_bf16 v[196:199], v[44:47], v[76:79], v[196:199]\n ds_read_b64_tr_b16 v[36:37], v9 offset:0\n" \
  "ds_read_b64_tr_b16 v[38:39], v9 offset:1024\n" \
  "v_mfma_f32_16x16x32_bf16 v[200:203], v[44:47], v[80:83], v[200:203]\n" \
  "v_mfma_f32_16x16x32_bf16 v[204:207], v[44:47], v[84:87], v[204:207]\n s_waitcnt lgkmcnt(12)\n" \
  "v_mfma_f32_16x16x32_bf16 v[208:211], v[48:51], v[76:79], v[208:211]\n ds_read_b64_tr_b16 v[40:41], v10 offset:0\n" \
  "ds_read_b64_tr_b16 v[42:43], v10 offset:1024\n" \
  "v_mfma_f32_16x16x32_bf16 v[212:215], v[48:51], v[80:83], v[212:215]\n" \
  "v_mfma_f32_16x16x32_bf16 v[216:219], v[48:51], v[84:87], v[216:219]\n" \
  "v_mfma_f32_16x16x32_bf16 v[220:223], v[52:55], v[76:79], v[220:223]\n ds_read_b64_tr_b16 v[44:45], v11 offset:0\n" \
  "ds_read_b64_tr_b16 v[46:47], v11 offset:1024\n" \
  "v_mfma_f32_16x16x32_bf16 v[224:227], v[52:55], v[80:83], v[224:227]\n s_add_u32 s64, s64, 1\n s_min_u32 s64, s64, 10\n" \
  "v_mfma_f32_16x16x32_bf16 v[228:231], v[52:55], v[84:87], v[228:231]\n s_waitcnt lgkmcnt(12)\n" \
  "v_mfma_f32_16x16x32_bf16 v[232:235], v[56:59], v[76:79], v[232:235]\n ds_read_b64_tr_b16 v[48:49], v12 offset:0\n" \
  "ds_read_b64_tr_b16 v[50:51], v12 offset:1024\n" \
  "v_mfma_f32_16x16x32_bf16 v[236:239], v[56:59], v[80:83], v[236:239]\n s_add_u32 s65, s65, 1\n s_cmp_lg_u32 s65, 11\n" \
  "v_mfma_f32_16x16x32_bf16 v[240:243], v[56:59], v[84:87], v[240:243]\n" \
  "v_mfma_f32_16x16x32_bf16 v[244:247], v[60:63], v[76:79], v[244:247]\n ds_read_b64_tr_b16 v[52:53], v13 offset:0\n" \
  "ds_read_b64_tr_b16 v[54:55], v13 offset:1024\n" \
  "v_mfma_f32_16x16x32_bf16 v[248:251], v[60:63], v[80:83], v[248:251]\n" \
  "v_mfma_f32_16x16x32_bf16 v[252:255], v[60:63], v[84:87], v[252:255]\n s_waitcnt lgkmcnt(8)\n" \
  ".endm\n" \
  ".macro QCVT \n" \
  "v_pk_mul_f32 v[160:161], v[160:161], s[68:69]\n v_pk_mul_f32 v[162:163], v[162:163], s[68:69]\n v_pk_mul_f32 v[164:165], v[164:165], s[68:69]\n" \
  "v_pk_mul_f32 v[166:167], v[166:167], s[68:69]\n v_pk_mul_f32 v[168:169], v[168:169], s[68:69]\n v_pk_mul_f32 v[170:171], v[170:171], s[68:69]\n" \
  "v_pk_mul_f32 v[172:173], v[172:173], s[68:69]\n v_pk_mul_f32 v[174:175], v[174:175], s[68:69]\n v_pk_mul_f32 v[176:177], v[176:177], s[68:69]\n" \
  "v_pk_mul_f32 v[178:179], v[178:179], s[68:69]\n v_pk_mul_f32 v[180:181], v[180:181], s[68:69]\n v_pk_mul_f32 v[182:183], v[182:183], s[68:69]\n" \
  "v_cvt_pk_bf16_f32 v112, v160, v161\n v_cvt_pk_bf16_f32 v114, v172, v173\n v_cvt_pk_bf16_f32 v113, v162, v163\n v_cvt_pk_bf16_f32 v115, v174, v175\n" \
  "v_cvt_pk_bf16_f32 v116, v164, v165\n v_cvt_pk_bf16_f32 v118, v176, v177\n v_cvt_pk_bf16_f32 v117, v166, v167\n v_cvt_pk_bf16_f32 v119, v178, v179\n" \
  "v_cvt_pk_bf16_f32 v120, v168, v169\n v_cvt_pk_bf16_f32 v122, v180, v181\n v_cvt_pk_bf16_f32 v121, v170, v171\n v_cvt_pk_bf16_f32 v123, v182, v183\n" \
  "v_permlane32_swap_b32 v112, v114\n v_permlane32_swap_b32 v113, v115\n v_permlane32_swap_b32 v116, v118\n v_permlane32_swap_b32 v117, v119\n" \
  "v_permlane32_swap_b32 v120, v122\n v_permlane32_swap_b32 v121, v123\n v_permlane16_swap_b32 v112, v114\n v_permlane16_swap_b32 v113, v115\n" \
  "v_permlane16_swap_b32 v116, v118\n v_permlane16_swap_b32 v117, v119\n v_permlane16_swap_b32 v120, v122\n v_permlane16_swap_b32 v121, v123\n" \
  "v_pk_mul_f32 v[184:185], v[184:185], s[68:69]\n v_pk_mul_f32 v[186:187], v[186:187], s[68:69]\n v_pk_mul_f32 v[188:189], v[188:189], s[68:69]\n" \
  "v_pk_mul_f32 v[190:191], v[190:191], s[68:69]\n v_pk_mul_f32 v[192:193], v[192:193], s[68:69]\n v_pk_mul_f32 v[194:195], v[194:195], s[68:69]\n" \
  "v_pk_mul_f32 v[196:197], v[196:197], s[68:69]\n v_pk_mul_f32 v[198:199], v[198:199], s[68:69]\n v_pk_mul_f32 v[200:201], v[200:201], s[68:69]\n" \
  "v_pk_mul_f32 v[202:203], v[202:203], s[68:69]\n v_pk_mul_f32 v[204:205], v[204:205], s[68:69]\n v_pk_mul_f32 v[206:207], v[206:207], s[68:69]\n" \
  "v_cvt_pk_bf16_f32 v124, v184, v185\n v_cvt_pk_bf16_f32 v126, v196, v197\n v_cvt_pk_bf16_f32 v125, v186, v187\n v_cvt_pk_bf16_f32 v127, v198, v199\n" \
  "v_cvt_pk_bf16_f32 v128, v188, v189\n v_cvt_pk_bf16_f32 v130, v200, v201\n v_cvt_pk_bf16_f32 v129, v190, v191\n v_cvt_pk_bf16_f32 v131, v202, v203\n" \
  "v_cvt_pk_bf16_f32 v132, v192, v193\n v_cvt_pk_bf16_f32 v134, v204, v205\n v_cvt_pk_bf16_f32 v133, v194, v195\n v_cvt_pk_bf16_f32 v135, v206, v207\n" \
  "v_permlane32_swap_b32 v124, v126\n v_permlane32_swap_b32 v125, v127\n v_permlane32_swap_b32 v128, v130\n v_permlane32_swap_b32 v129, v131\n" \
  "v_permlane32_swap_b32 v132, v134\n v_permlane32_swap_b32 v133, v135\n v_permlane16_swap_b32 v124, v126\n v_permlane16_swap_b32 v125, v127\n" \
  "v_permlane16_swap_b32 v128, v130\n v_permlane16_swap_b32 v129, v131\n v_permlane16_swap_b32 v132, v134\n v_permlane16_swap_b32 v133, v135\n" \
  "v_pk_mul_f32 v[208:209], v[208:209], s[68:69]\n v_pk_mul_f32 v[210:211], v[210:211], s[68:69]\n v_pk_mul_f32 v[212:213], v[212:213], s[68:69]\n" \
  "v_pk_mul_f32 v[214:215], v[214:215], s[68:69]\n v_pk_mul_f32 v[216:217], v[216:217], s[68:69]\n v_pk_mul_f32 v[218:219], v[218:219], s[68:69]\n" \
  "v_pk_mul_f32 v[220:221], v[220:221], s[68:69]\n v_pk_mul_f32 v[222:223], v[222:223], s[68:69]\n v_pk_mul_f32 v[224:225], v[224:225], s[68:69]\n" \
  "v_pk_mul_f32 v[226:227], v[226:227], s[68:69]\n v_pk_mul_f32 v[228:229], v[228:229], s[68:69]\n v_pk_mul_f32 v[230:231], v[230:231], s[68:69]\n" \
  "v_cvt_pk_bf16_f32 v136, v208, v209\n v_cvt_pk_bf16_f32 v138, v220, v221\n v_cvt_pk_bf16_f32 v137, v210, v211\n v_cvt_pk_bf16_f32 v139, v222, v223\n" \
  "v_cvt_pk_bf16_f32 v140, v212, v213\n v_cvt_pk_bf16_f32 v142, v224, v225\n v_cvt_pk_bf16_f32 v141, v214, v215\n v_cvt_pk_bf16_f32 v143, v226, v227\n" \
  "v_cvt_pk_bf16_f32 v144, v216, v217\n v_cvt_pk_bf16_f32 v146, v228, v229\n v_cvt_pk_bf16_f32 v145, v218, v219\n v_cvt_pk_bf16_f32 v147, v230, v231\n" \
  "v_permlane32_swap_b32 v136, v138\n v_permlane32_swap_b32 v137, v139\n v_permlane32_swap_b32 v140, v142\n v_permlane32_swap_b32 v141, v143\n" \
  "v_permlane32_swap_b32 v144, v146\n v_permlane32_swap_b32 v145, v147\n v_permlane16_swap_b32 v136, v138\n v_permlane16_swap_b32 v137, v139\n" \
  "v_permlane16_swap_b32 v140, v142\n v_permlane16_swap_b32 v141, v143\n v_permlane16_swap_b32 v144, v146\n v_permlane16_swap_b32 v145, v147\n" \
  "v_pk_mul_f32 v[232:233], v[232:233], s[68:69]\n v_pk_mul_f32 v[234:235], v[234:235], s[68:69]\n v_pk_mul_f32 v[236:237], v[236:237], s[68:69]\n" \
  "v_pk_mul_f32 v[238:239], v[238:239], s[68:69]\n v_pk_mul_f32 v[240:241], v[240:241], s[68:69]\n v_pk_mul_f32 v[242:243], v[242:243], s[68:69]\n" \
  "v_pk_mul_f32 v[244:245], v[244:245], s[68:69]\n v_pk_mul_f32 v[246:247], v[246:247], s[68:69]\n v_pk_mul_f32 v[248:249], v[248:249], s[68:69]\n" \
  "v_pk_mul_f32 v[250:251], v[250:251], s[68:69]\n v_pk_mul_f32 v[252:253], v[252:253], s[68:69]\n v_pk_mul_f32 v[254:255], v[254:255], s[68:69]\n" \
  "v_cvt_pk_bf16_f32 v148, v232, v233\n v_cvt_pk_bf16_f32 v150, v244, v245\n v_cvt_pk_bf16_f32 v149, v234, v235\n v_cvt_pk_bf16_f32 v151, v246, v247\n" \
  "v_cvt_pk_bf16_f32 v152, v236, v237\n v_cvt_pk_bf16_f32 v154, v248, v249\n v_cvt_pk_bf16_f32 v153, v238, v239\n v_cvt_pk_bf16_f32 v155, v250, v251\n" \
  "v_cvt_pk_bf16_f32 v156, v240, v241\n v_cvt_pk_bf16_f32 v158, v252, v253\n v_cvt_pk_bf16_f32 v157, v242, v243\n v_cvt_pk_bf16_f32 v159, v254, v255\n" \
  "v_permlane32_swap_b32 v148, v150\n v_permlane32_swap_b32 v149, v151\n v_permlane32_swap_b32 v152, v154\n v_permlane32_swap_b32 v153, v155\n" \
  "v_permlane32_swap_b32 v156, v158\n v_permlane32_swap_b32 v157, v159\n v_permlane16_swap_b32 v148, v150\n v_permlane16_swap_b32 v149, v151\n" \
  "v_permlane16_swap_b32 v152, v154\n v_permlane16_swap_b32 v153, v155\n v_permlane16_swap_b32 v156, v158\n v_permlane16_swap_b32 v157, v159\n" \
  ".endm\n" \
  ".macro FIRST so, sn, oc, on\n" \
  "ds_read_b128 v[32:35], v0 offset:\\oc+0\n ds_read_b128 v[36:39], v0 offset:\\oc+4608\n ds_read_b128 v[40:43], v0 offset:\\oc+64\n" \
  "ds_read_b128 v[44:47], v0 offset:\\oc+4672\n ds_read_b128 v[48:51], v0 offset:\\oc+128\n v_mov_b64 v[160:161], 0\n v_mov_b64 v[162:163], 0\n" \
  "v_mov_b64 v[164:165], 0\n v_mov_b64 v[166:167], 0\n v_mov_b64 v[168:169], 0\n v_mov_b64 v[170:171], 0\n v_mov_b64 v[172:173], 0\n" \
  "v_mov_b64 v[174:175], 0\n s_waitcnt lgkmcnt(3)\n" \
  "v_mfma_f32_16x16x32_bf16 v[\\sn+0:\\sn+3], v[32:35], v[112:115], 0\n ds_read_b128 v[52:55], v0 offset:\\oc+4736\n v_mov_b64 v[176:177], 0\n" \
  "v_mfma_f32_16x16x32_bf16 v[\\sn+8:\\sn+11], v[32:35], v[116:119], 0\n v_mov_b64 v[178:179], 0\n v_mov_b64 v[180:181], 0\n" \
  "v_mfma_f32_16x16x32_bf16 v[\\sn+16:\\sn+19], v[32:35], v[120:123], 0\n v_mov_b64 v[182:183], 0\n v_mov_b64 v[184:185], 0\n" \
  "v_mfma_f32_16x16x32_bf16 v[\\sn+4:\\sn+7], v[36:39], v[112:115], 0\n ds_read_b128 v[56:59], v0 offset:\\oc+192\n v_mov_b64 v[186:187], 0\n" \
  "v_mfma_f32_16x16x32_bf16 v[\\sn+12:\\sn+15], v[36:39], v[116:119], 0\n v_mov_b64 v[188:189], 0\n v_mov_b64 v[190:191], 0\n" \
  "v_mfma_f32_16x16x32_bf16 v[\\sn+20:\\sn+23], v[36:39], v[120:123], 0\n v_mov_b64 v[192:193], 0\n s_waitcnt lgkmcnt(3)\n" \
  "v_mfma_f32_16x16x32_bf16 v[\\sn+0:\\sn+3], v[40:43], v[124:127], v[\\sn+0:\\sn+3]\n ds_read_b128 v[60:63], v0 offset:\\oc+4800\n" \
  "v_mov_b64 v[194:195], 0\n" \
  "v_mfma_f32_16x16x32_bf16 v[\\sn+8:\\sn+11], v[40:43], v[128:131], v[\\sn+8:\\sn+11]\n v_mov_b64 v[196:197], 0\n v_mov_b64 v[198:199], 0\n" \
  "v_mfma_f32_16x16x32_bf16 v[\\sn+16:\\sn+19], v[40:43], v[132:135], v[\\sn+16:\\sn+19]\n v_mov_b64 v[200:201], 0\n v_mov_b64 v[202:203], 0\n" \
  "v_mfma_f32_16x16x32_bf16 v[\\sn+4:\\sn+7], v[44:47], v[124:127], v[\\sn+4:\\sn+7]\n v_mov_b64 v[204:205], 0\n v_mov_b64 v[206:207], 0\n" \
  "v_mfma_f32_16x16x32_bf16 v[\\sn+12:\\sn+15], v[44:47], v[128:131], v[\\sn+12:\\sn+15]\n v_mov_b64 v[208:209], 0\n v_mov_b64 v[210:211], 0\n" \
  "v_mfma_f32_16x16x32_bf16 v[\\sn+20:\\sn+23], v[44:47], v[132:135], v[\\sn+20:\\sn+23]\n v_mov_b64 v[212:213], 0\n s_waitcnt lgkmcnt(2)\n" \
  "v_mfma_f32_16x16x32_bf16 v[\\sn+0:\\sn+3], v[48:51], v[136:139], v[\\sn+0:\\sn+3]\n v_mov_b64 v[214:215], 0\n v_mov_b64 v[216:217], 0\n" \
  "v_mfma_f32_16x16x32_bf16 v[\\sn+8:\\sn+11], v[48:51], v[140:143], v[\\sn+8:\\sn+11]\n v_mov_b64 v[218:219], 0\n v_mov_b64 v[220:221], 0\n" \
  "v_mfma_f32_16x16x32_bf16 v[\\sn+16:\\sn+19], v[48:51], v[144:147], v[\\sn+16:\\sn+19]\n v_mov_b64 v[222:223], 0\n v_mov_b64 v[224:225], 0\n" \
  "v_mfma_f32_16x16x32_bf16 v[\\sn+4:\\sn+7], v[52:55], v[136:139], v[\\sn+4:\\sn+7]\n v_mov_b64 v[226:227], 0\n v_mov_b64 v[228:229], 0\n" \
  "v_mfma_f32_16x16x32_bf16 v[\\sn+12:\\sn+15], v[52:55], v[140:143], v[\\sn+12:\\sn+15]\n v_mov_b64 v[230:231], 0\n v_mov_b64 v[232:233], 0\n" \
  "v_mfma_f32_16x16x32_bf16 v[\\sn+20:\\sn+23], v[52:55], v[144:147], v[\\sn+20:\\sn+23]\n v_mov_b64 v[234:235], 0\n s_waitcnt lgkmcnt(0)\n" \
  "v_mfma_f32_16x16x32_bf16 v[\\sn+0:\\sn+3], v[56:59], v[148:151], v[\\sn+0:\\sn+3]\n v_mov_b64 v[236:237], 0\n v_mov_b64 v[238:239], 0\n" \
  "v_mfma_f32_16x16x32_bf16 v[\\sn+8:\\sn+11], v[56:59], v[152:155], v[\\sn+8:\\sn+11]\n v_mov_b64 v[240:241], 0\n v_mov_b64 v[242:243], 0\n" \
  "v_mfma_f32_16x16x32_bf16 v[\\sn+16:\\sn+19], v[56:59], v[156:159], v[\\sn+16:\\sn+19]\n v_mov_b64 v[244:245], 0\n v_mov_b64 v[246:247], 0\n" \
  "v_mfma_f32_16x16x32_bf16 v[\\sn+4:\\sn+7], v[60:63], v[148:151], v[\\sn+4:\\sn+7]\n v_mov_b64 v[248:249], 0\n v_mov_b64 v[250:251], 0\n" \
  "v_mfma_f32_16x16x32_bf16 v[\\sn+12:\\sn+15], v[60:63], v[152:155], v[\\sn+12:\\sn+15]\n v_mov_b64 v[252:253], 0\n v_mov_b64 v[254:255], 0\n" \
  "v_mfma_f32_16x16x32_bf16 v[\\sn+20:\\sn+23], v[60:63], v[156:159], v[\\sn+20:\\sn+23]\n v_mov_b64 v[20:21], 0\n v_mov_b32 v[\\so+14], 11\n" \
  "ds_write_b8 v3, v[\\so+14]\n v_mov_b64 v[22:23], 0\n v_mov_b64 v[24:25], 0\n v_mov_b64 v[26:27], 0\n v_mov_b64 v[28:29], 0\n v_mov_b64 v[30:31], 0\n" \
  "v_mov_b32 v4, 0x3f803f80\n v_mov_b32 v5, 0x3f803f80\n v_mov_b32 v6, 0x3f803f80\n v_mov_b32 v7, 0x3f803f80\n" \
  "v_max3_f32 v[\\so+4], v[\\sn+0], v[\\sn+1], v[\\sn+2]\n v_max3_f32 v[\\so+4], v[\\so+4], v[\\sn+3], v[\\sn+4]\n" \
  "v_max3_f32 v[\\so+4], v[\\so+4], v[\\sn+5], v[\\sn+6]\n v_max3_f32 v[\\so+12], v[\\sn+8], v[\\sn+9], v[\\sn+10]\n" \
  "v_max3_f32 v[\\so+12], v[\\so+12], v[\\sn+11], v[\\sn+12]\n v_max3_f32 v[\\so+12], v[\\so+12], v[\\sn+13], v[\\sn+14]\n" \
  "v_max3_f32 v[\\so+20], v[\\sn+16], v[\\sn+17], v[\\sn+18]\n v_max3_f32 v[\\so+20], v[\\so+20], v[\\sn+19], v[\\sn+20]\n" \
  "v_max3_f32 v[\\so+20], v[\\so+20], v[\\sn+21], v[\\sn+22]\n REF \\so, \\sn, 1\n AWAIT \\so, 4\n ds_read_b128 v[32:35], v0 offset:\\on+0\n" \
  "ds_read_b128 v[36:39], v0 offset:\\on+4608\n ds_read_b128 v[40:43], v0 offset:\\on+64\n ds_read_b128 v[44:47], v0 offset:\\on+4672\n" \
  "ds_read_b128 v[48:51], v0 offset:\\on+128\n v_exp_f32 v[\\sn+0], v[\\sn+0]\n v_exp_f32 v[\\sn+1], v[\\sn+1]\n v_exp_f32 v[\\sn+2], v[\\sn+2]\n" \
  "v_exp_f32 v[\\sn+3], v[\\sn+3]\n v_exp_f32 v[\\sn+4], v[\\sn+4]\n v_exp_f32 v[\\sn+5], v[\\sn+5]\n v_exp_f32 v[\\sn+6], v[\\sn+6]\n" \
  "v_exp_f32 v[\\sn+7], v[\\sn+7]\n v_exp_f32 v[\\sn+8], v[\\sn+8]\n v_exp_f32 v[\\sn+9], v[\\sn+9]\n v_cvt_pk_bf16_f32 v[\\sn+0], v[\\sn+0], v[\\sn+1]\n" \
  "v_exp_f32 v[\\sn+10], v[\\sn+10]\n v_exp_f32 v[\\sn+11], v[\\sn+11]\n s_waitcnt lgkmcnt(3)\n" \
  ".endm\n" \
  ".macro ITER id, so, sn, oc, on\n" \
  "v_mfma_f32_16x16x32_bf16 v[\\sn+0:\\sn+3], v[32:35], v[112:115], v[8:11]\n ds_read_b128 v[52:55], v0 offset:\\oc+4736\n" \
  "v_cvt_pk_bf16_f32 v[\\so+1], v[\\so+2], v[\\so+3]\n" \
  "v_mfma_f32_16x16x32_bf16 v[\\sn+8:\\sn+11], v[32:35], v[116:119], v[12:15]\n v_exp_f32 v[\\so+12], v[\\so+12]\n" \
  "v_mfma_f32_16x16x32_bf16 v[\\sn+16:\\sn+19], v[32:35], v[120:123], v[16:19]\n v_exp_f32 v[\\so+13], v[\\so+13]\n" \
  "v_mfma_f32_16x16x32_bf16 v[\\sn+4:\\sn+7], v[36:39], v[112:115], v[8:11]\n ds_read_b128 v[56:59], v0 offset:\\oc+192\n" \
  "v_cvt_pk_bf16_f32 v[\\so+2], v[\\so+4], v[\\so+5]\n" \
  "v_mfma_f32_16x16x32_bf16 v[\\sn+12:\\sn+15], v[36:39], v[116:119], v[12:15]\n v_exp_f32 v[\\so+14], v[\\so+14]\n" \
  "v_mfma_f32_16x16x32_bf16 v[\\sn+20:\\sn+23], v[36:39], v[120:123], v[16:19]\n v_exp_f32 v[\\so+15], v[\\so+15]\n s_waitcnt lgkmcnt(3)\n" \
  "v_mfma_f32_16x16x32_bf16 v[\\sn+0:\\sn+3], v[40:43], v[124:127], v[\\sn+0:\\sn+3]\n ds_read_b128 v[60:63], v0 offset:\\oc+4800\n" \
  "v_cvt_pk_bf16_f32 v[\\so+3], v[\\so+6], v[\\so+7]\n" \
  "v_mfma_f32_16x16x32_bf16 v[\\sn+8:\\sn+11], v[40:43], v[128:131], v[\\sn+8:\\sn+11]\n v_exp_f32 v[\\so+16], v[\\so+16]\n" \
  "v_mfma_f32_16x16x32_bf16 v[\\sn+16:\\sn+19], v[40:43], v[132:135], v[\\sn+16:\\sn+19]\n v_exp_f32 v[\\so+17], v[\\so+17]\n" \
  "v_mfma_f32_16x16x32_bf16 v[\\sn+4:\\sn+7], v[44:47], v[124:127], v[\\sn+4:\\sn+7]\n ds_read_b64_tr_b16 v[32:33], v1 offset:\\oc+0\n" \
  "ds_read_b64_tr_b16 v[34:35], v1 offset:\\oc+4608\n" \
  "v_mfma_f32_16x16x32_bf16 v[\\sn+12:\\sn+15], v[44:47], v[128:131], v[\\sn+12:\\sn+15]\n v_cvt_pk_bf16_f32 v[\\so+8], v[\\so+8], v[\\so+9]\n" \
  "v_exp_f32 v[\\so+18], v[\\so+18]\n" \
  "v_mfma_f32_16x16x32_bf16 v[\\sn+20:\\sn+23], v[44:47], v[132:135], v[\\sn+20:\\sn+23]\n v_exp_f32 v[\\so+19], v[\\so+19]\n s_waitcnt lgkmcnt(4)\n" \
  "v_mfma_f32_16x16x32_bf16 v[\\sn+0:\\sn+3], v[48:51], v[136:139], v[\\sn+0:\\sn+3]\n ds_read_b64_tr_b16 v[36:37], v1 offset:\\oc+32\n" \
  "ds_read_b64_tr_b16 v[38:39], v1 offset:\\oc+4640\n" \
  "v_mfma_f32_16x16x32_bf16 v[\\sn+8:\\sn+11], v[48:51], v[140:143], v[\\sn+8:\\sn+11]\n v_cvt_pk_bf16_f32 v[\\so+9], v[\\so+10], v[\\so+11]\n" \
  "v_cvt_pk_bf16_f32 v[\\so+16], v[\\so+16], v[\\so+17]\n" \
  "v_mfma_f32_16x16x32_bf16 v[\\sn+16:\\sn+19], v[48:51], v[144:147], v[\\sn+16:\\sn+19]\n v_exp_f32 v[\\so+20], v[\\so+20]\n" \
  "v_mfma_f32_16x16x32_bf16 v[\\sn+4:\\sn+7], v[52:55], v[136:139], v[\\sn+4:\\sn+7]\n ds_read_b64_tr_b16 v[40:41], v1 offset:\\oc+64\n" \
  "ds_read_b64_tr_b16 v[42:43], v1 offset:\\oc+4672\n" \
  "v_mfma_f32_16x16x32_bf16 v[\\sn+12:\\sn+15], v[52:55], v[140:143], v[\\sn+12:\\sn+15]\n v_exp_f32 v[\\so+21], v[\\so+21]\n" \
  "v_mfma_f32_16x16x32_bf16 v[\\sn+20:\\sn+23], v[52:55], v[144:147], v[\\sn+20:\\sn+23]\n v_cvt_pk_bf16_f32 v[\\so+10], v[\\so+12], v[\\so+13]\n" \
  "s_waitcnt lgkmcnt(6)\n" \
  "v_mfma_f32_16x16x32_bf16 v[\\sn+0:\\sn+3], v[56:59], v[148:151], v[\\sn+0:\\sn+3]\n ds_read_b64_tr_b16 v[44:45], v1 offset:\\oc+96\n" \
  "ds_read_b64_tr_b16 v[46:47], v1 offset:\\oc+4704\n" \
  "v_mfma_f32_16x16x32_bf16 v[\\sn+8:\\sn+11], v[56:59], v[152:155], v[\\sn+8:\\sn+11]\n v_cvt_pk_bf16_f32 v[\\so+17], v[\\so+18], v[\\so+19]\n" \
  "v_exp_f32 v[\\so+22], v[\\so+22]\n" \
  "v_mfma_f32_16x16x32_bf16 v[\\sn+16:\\sn+19], v[56:59], v[156:159], v[\\sn+16:\\sn+19]\n v_exp_f32 v[\\so+23], v[\\so+23]\n" \
  "v_mfma_f32_16x16x32_bf16 v[\\sn+4:\\sn+7], v[60:63], v[148:151], v[\\sn+4:\\sn+7]\n ds_read_b64_tr_b16 v[48:49], v1 offset:\\oc+128\n" \
  "ds_read_b64_tr_b16 v[50:51], v1 offset:\\oc+4736\n v_cvt_pk_bf16_f32 v[\\so+11], v[\\so+14], v[\\so+15]\n" \
  "v_mfma_f32_16x16x32_bf16 v[\\sn+12:\\sn+15], v[60:63], v[152:155], v[\\sn+12:\\sn+15]\n v_cvt_pk_bf16_f32 v[\\so+18], v[\\so+20], v[\\so+21]\n" \
  "v_cvt_pk_bf16_f32 v[\\so+19], v[\\so+22], v[\\so+23]\n" \
  "v_mfma_f32_16x16x32_bf16 v[\\sn+20:\\sn+23], v[60:63], v[156:159], v[\\sn+20:\\sn+23]\n s_waitcnt lgkmcnt(6)\n" \
  "v_mfma_f32_16x16x32_bf16 v[160:163], v[32:35], v[\\so+0:\\so+3], v[160:163]\n ds_read_b64_tr_b16 v[52:53], v1 offset:\\oc+160\n" \
  "ds_read_b64_tr_b16 v[54:55], v1 offset:\\oc+4768\n" \
  "v_mfma_f32_16x16x32_bf16 v[164:167], v[32:35], v[\\so+8:\\so+11], v[164:167]\n ds_read_u8 v[\\so+6], v2 offset:4\n v_mov_b32 v[\\so+14], s65\n" \
  "v_mfma_f32_16x16x32_bf16 v[168:171], v[32:35], v[\\so+16:\\so+19], v[168:171]\n v_max3_f32 v[\\so+4], v[\\sn+0], v[\\sn+1], v[\\sn+2]\n" \
  "v_max3_f32 v[\\so+12], v[\\sn+8], v[\\sn+9], v[\\sn+10]\n v_max3_f32 v[\\so+20], v[\\sn+16], v[\\sn+17], v[\\sn+18]\n" \
  "v_mfma_f32_16x16x32_bf16 v[172:175], v[36:39], v[\\so+0:\\so+3], v[172:175]\n ds_read_b64_tr_b16 v[56:57], v1 offset:\\oc+192\n" \
  "ds_read_b64_tr_b16 v[58:59], v1 offset:\\oc+4800\n v_max3_f32 v[\\so+4], v[\\so+4], v[\\sn+3], v[\\sn+4]\n" \
  "v_mfma_f32_16x16x32_bf16 v[176:179], v[36:39], v[\\so+8:\\so+11], v[176:179]\n v_max3_f32 v[\\so+12], v[\\so+12], v[\\sn+11], v[\\sn+12]\n" \
  "v_max3_f32 v[\\so+20], v[\\so+20], v[\\sn+19], v[\\sn+20]\n" \
  "v_mfma_f32_16x16x32_bf16 v[180:183], v[36:39], v[\\so+16:\\so+19], v[180:183]\n v_max3_f32 v[\\so+4], v[\\so+4], v[\\sn+5], v[\\sn+6]\n" \
  "s_waitcnt lgkmcnt(7)\n" \
  "v_mfma_f32_16x16x32_bf16 v[184:187], v[40:43], v[\\so+0:\\so+3], v[184:187]\n ds_read_b64_tr_b16 v[60:61], v1 offset:\\oc+224\n" \
  "ds_read_b64_tr_b16 v[62:63], v1 offset:\\oc+4832\n" \
  "v_mfma_f32_16x16x32_bf16 v[188:191], v[40:43], v[\\so+8:\\so+11], v[188:191]\n ds_write_b8 v3, v[\\so+14]\n .if \\id == 0\n v_add_u32 v0, s66, v0\n" \
  "v_add_u32 v1, s66, v1\n s_sub_u32 s66, 0, s66\n .endif\n" \
  "v_mfma_f32_16x16x32_bf16 v[192:195], v[40:43], v[\\so+16:\\so+19], v[192:195]\n s_waitcnt lgkmcnt(5)\n v_cmp_gt_u32 vcc, s64, v[\\so+6]\n" \
  "s_cbranch_vccnz .Lw\\id\\()_%=\n .Lwr\\id\\()_%=:\n" \
  "v_mfma_f32_16x16x32_bf16 v[196:199], v[44:47], v[\\so+0:\\so+3], v[196:199]\n ds_read_b128 v[32:35], v0 offset:\\on+0\n" \
  "v_max3_f32 v[\\so+12], v[\\so+12], v[\\sn+13], v[\\sn+14]\n" \
  "v_mfma_f32_16x16x32_bf16 v[200:203], v[44:47], v[\\so+8:\\so+11], v[200:203]\n v_max3_f32 v[\\so+20], v[\\so+20], v[\\sn+21], v[\\sn+22]\n" \
  "v_max3_f32 v[\\so+13], v[\\sn+7], v[\\sn+15], v[\\sn+23]\n" \
  "v_mfma_f32_16x16x32_bf16 v[204:207], v[44:47], v[\\so+16:\\so+19], v[204:207]\n v_max3_f32 v[\\so+5], v[\\so+4], v[\\so+12], v[\\so+20]\n" \
  "s_waitcnt lgkmcnt(7)\n" \
  "v_mfma_f32_16x16x32_bf16 v[208:211], v[48:51], v[\\so+0:\\so+3], v[208:211]\n ds_read_b128 v[36:39], v0 offset:\\on+4608\n" \
  "v_max_f32 v[\\so+5], v[\\so+5], v[\\so+13]\n" \
  "v_mfma_f32_16x16x32_bf16 v[212:215], v[48:51], v[\\so+8:\\so+11], v[212:215]\n v_cmp_lt_f32 vcc, 0x41000000, v[\\so+5]\n" \
  "s_cbranch_vccnz .Lm\\id\\()_%=\n .Lmr\\id\\()_%=:\n" \
  "v_mfma_f32_16x16x32_bf16 v[216:219], v[48:51], v[\\so+16:\\so+19], v[216:219]\n s_add_u32 s65, s65, 1\n v_exp_f32 v[\\sn+0], v[\\sn+0]\n" \
  "v_mfma_f32_16x16x32_bf16 v[220:223], v[52:55], v[\\so+0:\\so+3], v[220:223]\n ds_read_b128 v[40:43], v0 offset:\\on+64\n s_add_u32 s64, s64, 2\n" \
  "s_min_u32 s64, s64, 32\n" \
  "v_mfma_f32_16x16x32_bf16 v[224:227], v[52:55], v[\\so+8:\\so+11], v[224:227]\n v_exp_f32 v[\\sn+1], v[\\sn+1]\n" \
  "v_mfma_f32_16x16x32_bf16 v[228:231], v[52:55], v[\\so+16:\\so+19], v[228:231]\n v_exp_f32 v[\\sn+2], v[\\sn+2]\n" \
  "v_mfma_f32_16x16x32_bf16 v[20:23], v[4:7], v[\\so+0:\\so+3], v[20:23]\n v_exp_f32 v[\\sn+3], v[\\sn+3]\n s_waitcnt lgkmcnt(4)\n" \
  "v_mfma_f32_16x16x32_bf16 v[232:235], v[56:59], v[\\so+0:\\so+3], v[232:235]\n ds_read_b128 v[44:47], v0 offset:\\on+4672\n" \
  "v_exp_f32 v[\\sn+4], v[\\sn+4]\n" \
  "v_mfma_f32_16x16x32_bf16 v[236:239], v[56:59], v[\\so+8:\\so+11], v[236:239]\n v_exp_f32 v[\\sn+5], v[\\sn+5]\n" \
  "v_mfma_f32_16x16x32_bf16 v[240:243], v[56:59], v[\\so+16:\\so+19], v[240:243]\n v_exp_f32 v[\\sn+6], v[\\sn+6]\n" \
  "v_mfma_f32_16x16x32_bf16 v[24:27], v[4:7], v[\\so+8:\\so+11], v[24:27]\n v_exp_f32 v[\\sn+7], v[\\sn+7]\n" \
  "v_mfma_f32_16x16x32_bf16 v[244:247], v[60:63], v[\\so+0:\\so+3], v[244:247]\n ds_read_b128 v[48:51], v0 offset:\\on+128\n" \
  "v_exp_f32 v[\\sn+8], v[\\sn+8]\n" \
  "v_mfma_f32_16x16x32_bf16 v[248:251], v[60:63], v[\\so+8:\\so+11], v[248:251]\n v_exp_f32 v[\\sn+9], v[\\sn+9]\n" \
  "v_mfma_f32_16x16x32_bf16 v[252:255], v[60:63], v[\\so+16:\\so+19], v[252:255]\n v_cvt_pk_bf16_f32 v[\\sn+0], v[\\sn+0], v[\\sn+1]\n" \
  "v_exp_f32 v[\\sn+10], v[\\sn+10]\n" \
  "v_mfma_f32_16x16x32_bf16 v[28:31], v[4:7], v[\\so+16:\\so+19], v[28:31]\n v_exp_f32 v[\\sn+11], v[\\sn+11]\n s_waitcnt lgkmcnt(3)\n" \
  "s_cbranch_vccnz .Lr\\id\\()_%=\n .Lrr\\id\\()_%=:\n" \
  ".endm\n" \
  ".macro LAST so, oc\n" \
  "ds_read_b64_tr_b16 v[32:33], v1 offset:\\oc+0\n ds_read_b64_tr_b16 v[34:35], v1 offset:\\oc+4608\n ds_read_b64_tr_b16 v[36:37], v1 offset:\\oc+32\n" \
  "ds_read_b64_tr_b16 v[38:39], v1 offset:\\oc+4640\n ds_read_b64_tr_b16 v[40:41], v1 offset:\\oc+64\n ds_read_b64_tr_b16 v[42:43], v1 offset:\\oc+4672\n" \
  "ds_read_b64_tr_b16 v[44:45], v1 offset:\\oc+96\n ds_read_b64_tr_b16 v[46:47], v1 offset:\\oc+4704\n ds_read_b64_tr_b16 v[48:49], v1 offset:\\oc+128\n" \
  "ds_read_b64_tr_b16 v[50:51], v1 offset:\\oc+4736\n v_cvt_pk_bf16_f32 v[\\so+1], v[\\so+2], v[\\so+3]\n v_exp_f32 v[\\so+12], v[\\so+12]\n" \
  "v_exp_f32 v[\\so+13], v[\\so+13]\n v_cvt_pk_bf16_f32 v[\\so+2], v[\\so+4], v[\\so+5]\n v_exp_f32 v[\\so+14], v[\\so+14]\n" \
  "v_exp_f32 v[\\so+15], v[\\so+15]\n v_cvt_pk_bf16_f32 v[\\so+3], v[\\so+6], v[\\so+7]\n v_exp_f32 v[\\so+16], v[\\so+16]\n" \
  "v_exp_f32 v[\\so+17], v[\\so+17]\n v_cvt_pk_bf16_f32 v[\\so+8], v[\\so+8], v[\\so+9]\n v_exp_f32 v[\\so+18], v[\\so+18]\n" \
  "v_exp_f32 v[\\so+19], v[\\so+19]\n v_cvt_pk_bf16_f32 v[\\so+9], v[\\so+10], v[\\so+11]\n v_cvt_pk_bf16_f32 v[\\so+16], v[\\so+16], v[\\so+17]\n" \
  "v_exp_f32 v[\\so+20], v[\\so+20]\n v_exp_f32 v[\\so+21], v[\\so+21]\n v_cvt_pk_bf16_f32 v[\\so+10], v[\\so+12], v[\\so+13]\n" \
  "v_cvt_pk_bf16_f32 v[\\so+17], v[\\so+18], v[\\so+19]\n v_exp_f32 v[\\so+22], v[\\so+22]\n v_exp_f32 v[\\so+23], v[\\so+23]\n" \
  "v_cvt_pk_bf16_f32 v[\\so+11], v[\\so+14], v[\\so+15]\n v_cvt_pk_bf16_f32 v[\\so+18], v[\\so+20], v[\\so+21]\n s_nop 0\n" \
  "v_cvt_pk_bf16_f32 v[\\so+19], v[\\so+22], v[\\so+23]\n s_waitcnt lgkmcnt(6)\n s_nop 0\n" \
  "v_mfma_f32_16x16x32_bf16 v[160:163], v[32:35], v[\\so+0:\\so+3], v[160:163]\n ds_read_b64_tr_b16 v[52:53], v1 offset:\\oc+160\n" \
  "ds_read_b64_tr_b16 v[54:55], v1 offset:\\oc+4768\n" \
  "v_mfma_f32_16x16x32_bf16 v[164:167], v[32:35], v[\\so+8:\\so+11], v[164:167]\n" \
  "v_mfma_f32_16x16x32_bf16 v[168:171], v[32:35], v[\\so+16:\\so+19], v[168:171]\n" \
  "v_mfma_f32_16x16x32_bf16 v[172:175], v[36:39], v[\\so+0:\\so+3], v[172:175]\n ds_read_b64_tr_b16 v[56:57], v1 offset:\\oc+192\n" \
  "ds_read_b64_tr_b16 v[58:59], v1 offset:\\oc+4800\n" \
  "v_mfma_f32_16x16x32_bf16 v[176:179], v[36:39], v[\\so+8:\\so+11], v[176:179]\n" \
  "v_mfma_f32_16x16x32_bf16 v[180:183], v[36:39], v[\\so+16:\\so+19], v[180:183]\n s_waitcnt lgkmcnt(6)\n" \
  "v_mfma_f32_16x16x32_bf16 v[184:187], v[40:43], v[\\so+0:\\so+3], v[184:187]\n ds_read_b64_tr_b16 v[60:61], v1 offset:\\oc+224\n" \
  "ds_read_b64_tr_b16 v[62:63], v1 offset:\\oc+4832\n" \
  "v_mfma_f32_16x16x32_bf16 v[188:191], v[40:43], v[\\so+8:\\so+11], v[188:191]\n" \
  "v_mfma_f32_16x16x32_bf16 v[192:195], v[40:43], v[\\so+16:\\so+19], v[192:195]\n" \
  "v_mfma_f32_16x16x32_bf16 v[196:199], v[44:47], v[\\so+0:\\so+3], v[196:199]\n" \
  "v_mfma_f32_16x16x32_bf16 v[200:203], v[44:47], v[\\so+8:\\so+11], v[200:203]\n" \
  "v_mfma_f32_16x16x32_bf16 v[204:207], v[44:47], v[\\so+16:\\so+19], v[204:207]\n s_waitcnt lgkmcnt(4)\n" \
  "v_mfma_f32_16x16x32_bf16 v[208:211], v[48:51], v[\\so+0:\\so+3], v[208:211]\n" \
  "v_mfma_f32_16x16x32_bf16 v[212:215], v[48:51], v[\\so+8:\\so+11], v[212:215]\n" \
  "v_mfma_f32_16x16x32_bf16 v[216:219], v[48:51], v[\\so+16:\\so+19], v[216:219]\n" \
  "v_mfma_f32_16x16x32_bf16 v[220:223], v[52:55], v[\\so+0:\\so+3], v[220:223]\n" \
  "v_mfma_f32_16x16x32_bf16 v[224:227], v[52:55], v[\\so+8:\\so+11], v[224:227]\n" \
  "v_mfma_f32_16x16x32_bf16 v[228:231], v[52:55], v[\\so+16:\\so+19], v[228:231]\n" \
  "v_mfma_f32_16x16x32_bf16 v[20:23], v[4:7], v[\\so+0:\\so+3], v[20:23]\n s_waitcnt lgkmcnt(0)\n" \
  "v_mfma_f32_16x16x32_bf16 v[232:235], v[56:59], v[\\so+0:\\so+3], v[232:235]\n" \
  "v_mfma_f32_16x16x32_bf16 v[236:239], v[56:59], v[\\so+8:\\so+11], v[236:239]\n" \
  "v_mfma_f32_16x16x32_bf16 v[240:243], v[56:59], v[\\so+16:\\so+19], v[240:243]\n" \
  "v_mfma_f32_16x16x32_bf16 v[24:27], v[4:7], v[\\so+8:\\so+11], v[24:27]\n" \
  "v_mfma_f32_16x16x32_bf16 v[244:247], v[60:63], v[\\so+0:\\so+3], v[244:247]\n" \
  "v_mfma_f32_16x16x32_bf16 v[248:251], v[60:63], v[\\so+8:\\so+11], v[248:251]\n" \
  "v_mfma_f32_16x16x32_bf16 v[252:255], v[60:63], v[\\so+16:\\so+19], v[252:255]\n" \
  "v_mfma_f32_16x16x32_bf16 v[28:31], v[4:7], v[\\so+16:\\so+19], v[28:31]\n" \
  ".endm\n" \
  ""
#define ATT_ASM \
  ".macro AWAIT so, off\n" \
  "1:\n ds_read_u8 v[\\so+6], v2 offset:\\off\n s_waitcnt lgkmcnt(0)\n v_cmp_gt_u32 vcc, s64, v[\\so+6]\n s_cbranch_vccz 2f\n" \
  "s_sub_u32 s67, s67, 1\n s_cbranch_scc0 1b\n s_mov_b32 s67, 0\n 2:\n" \
  ".endm\n" \
  ".macro REF so, sn, first\n" \
  ".irp b, 0, 8, 16\n v_max_f32 v[\\so+\\b+4], v[\\so+\\b+4], v[\\sn+\\b+7]\n .endr\n" \
  ".irp b, 0, 8, 16\n v_mov_b32 v[\\so+\\b+5], v[\\so+\\b+4]\n .endr\n" \
  ".irp b, 0, 8, 16\n v_permlane16_swap_b32 v[\\so+\\b+4], v[\\so+\\b+5]\n .endr\n" \
  ".irp b, 0, 8, 16\n v_max_f32 v[\\so+\\b+4], v[\\so+\\b+4], v[\\so+\\b+5]\n .endr\n" \
  ".irp b, 0, 8, 16\n v_mov_b32 v[\\so+\\b+5], v[\\so+\\b+4]\n .endr\n" \
  ".irp b, 0, 8, 16\n v_permlane32_swap_b32 v[\\so+\\b+4], v[\\so+\\b+5]\n .endr\n" \
  ".irp b, 0, 8, 16\n" \
  ".if \\first\n v_max_f32 v[\\so+\\b+4], v[\\so+\\b+4], v[\\so+\\b+5]\n" \
  ".else\n v_max3_f32 v[\\so+\\b+4], v[\\so+\\b+4], v[\\so+\\b+5], 0\n v_exp_f32 v[\\so+\\b+7], -v[\\so+\\b+4]\n .endif\n" \
  ".irp k, 0, 1, 2, 3\n" \
  ".if \\first\n v_sub_f32 v[8+\\b/2+\\k], 0, v[\\so+\\b+4]\n .else\n v_sub_f32 v[8+\\b/2+\\k], v[8+\\b/2+\\k], v[\\so+\\b+4]\n .endif\n" \
  "v_sub_f32 v[\\sn+\\b+\\k], v[\\sn+\\b+\\k], v[\\so+\\b+4]\n v_sub_f32 v[\\sn+\\b+\\k+4], v[\\sn+\\b+\\k+4], v[\\so+\\b+4]\n" \
  ".endr\n .endr\n" \
  ".endm\n" \
  ".macro SIDE id, so, sn\n" \
  ".Lw\\id\\()_%=:\n AWAIT \\so, 4\n s_branch .Lwr\\id\\()_%=\n" \
  ".Lm\\id\\()_%=:\n REF \\so, \\sn, 0\n s_branch .Lmr\\id\\()_%=\n" \
  ".Lr\\id\\()_%=:\n s_nop 7\n" \
  ".set nn, 0\n .rept 96\n v_mul_f32 v[160+nn], v[160+nn], v[\\so+((nn/4)%%3)*8+7]\n .set nn, nn+1\n .endr\n" \
  ".set nn, 0\n .rept 12\n v_mul_f32 v[20+nn], v[20+nn], v[\\so+(nn/4)*8+7]\n .set nn, nn+1\n .endr\n" \
  "s_branch .Lrr\\id\\()_%=\n" \
  ".endm\n" \
  ATT_MACROS \
  "s_mov_b32 s64, 1\n s_mov_b32 s65, 1\n s_mov_b32 s67, 0x100000\n s_mov_b32 s68, 2\n s_mov_b32 s69, -81920\n s_mov_b32 s70, 40960\n" \
  ".set nn, 160\n .rept 48\n v_mov_b64 v[nn:nn+1], 0\n .set nn, nn+2\n .endr\n" \
  "global_load_dwordx4 v[20:23], v18, %[pc]\n" \
  "AWAIT 88, 0\n s_mov_b32 s64, 2\n QPRE\n" \
  ".Lq_%=:\n QSTAGE\n s_cbranch_scc1 .Lq_%=\n" \
  "s_mov_b32 s68, %[scl]\n s_mov_b32 s69, %[scl]\n s_mov_b32 s64, 1\n s_mov_b32 s65, 12\n s_mov_b32 s66, 81920\n s_waitcnt vmcnt(0)\n" \
  "QCVT\n AWAIT 88, 4\n s_mov_b32 s64, 3\n" \
  "FIRST 88, 64, 0, 40960\n s_mov_b32 s64, 5\n" \
  ".Lat_%=:\n" \
  "ITER 0, 64, 88, 40960, 0\n" \
  "s_cmp_eq_u32 s65, 27\n s_cbranch_scc1 .Lla_%=\n" \
  "ITER 1, 88, 64, 0, 40960\n" \
  "s_branch .Lat_%=\n" \
  ".Lqw_%=:\n AWAIT 88, 0\n s_branch .Lqr_%=\n SIDE 0, 64, 88\n SIDE 1, 88, 64\n" \
  ".Lla_%=:\n" \
  "LAST 88, 0\n s_nop 7\n" \
  ".purgem AWAIT\n .purgem REF\n .purgem SIDE\n .purgem FIRST\n .purgem ITER\n .purgem LAST\n .purgem QPRE\n .purgem QSTAGE\n .purgem QCVT\n"

#define P2_BODY \
  "s_waitcnt vmcnt(12)\n WRD8 128, 0\n s_waitcnt lgkmcnt(0)\n WDMA 0\n" \
  "s_waitcnt vmcnt(12)\n WRD8 144, 1\n s_waitcnt lgkmcnt(0)\n WDMA 1\n" \
  "s_waitcnt vmcnt(12)\n WRD8 160, 2\n s_waitcnt lgkmcnt(0)\n WDMA 2\n" \
  "s_waitcnt vmcnt(12)\n WRD8 176, 3\n s_waitcnt lgkmcnt(0)\n WDMA 3\n" \
  "s_waitcnt vmcnt(12)\n WRD8 192, 0\n s_waitcnt lgkmcnt(0)\n WDMA 0\n" \
  "s_waitcnt vmcnt(12)\n WRD8 208, 1\n s_waitcnt lgkmcnt(0)\n WDMA 1\n" \
  "s_waitcnt vmcnt(12)\n WRD8 224, 2\n s_waitcnt lgkmcnt(0)\n WDMA 2\n" \
  "s_waitcnt vmcnt(12)\n WRD8 240, 3\n s_waitcnt lgkmcnt(0)\n WDMA 3\n" \
  "s_waitcnt vmcnt(12)\n WRD8 16, 0\n s_waitcnt lgkmcnt(0)\n WDMA 0\n" \
  "SPIN 0\n OLD 32\n OLD 48\n" \
  "s_waitcnt vmcnt(4)\n STEP 128, 32, 0, 2\n OLD 32\n FLG\n OLD 128\n" \
  "s_waitcnt vmcnt(9)\n STEP 144, 48, 0, 3\n CHK 1, 4\n OLD 48\n" \
  "s_waitcnt vmcnt(9)\n STEP 160, 32, 0, 0\n OLD 144\n" \
  "s_waitcnt vmcnt(8)\n STEP 176, 128, 0, 1\n OLD 32\n" \
  "s_waitcnt vmcnt(8)\n STEP 192, 48, 0, 2\n FLG\n OLD 160\n" \
  "s_waitcnt vmcnt(9)\n STEP 208, 144, 0, 3\n CHK 2, 4\n OLD 128\n" \
  "s_waitcnt vmcnt(9)\n STEP 224, 32, 0, 0\n OLD 176\n" \
  "s_waitcnt vmcnt(8)\n STEP 240, 160, 48, 1\n s_waitcnt lgkmcnt(0)\n WDMA 1\n OLD 192\n" \
  "s_waitcnt vmcnt(12)\n STEP 16, 128, 144, 2\n s_waitcnt lgkmcnt(0)\n WDMA 2\n FLG\n OLD 208\n" \
  "s_waitcnt vmcnt(17)\n STEP 48, 176, 32, 3\n s_waitcnt lgkmcnt(0)\n WDMA 3\n CHK 3, 8\n OLD 224\n" \
  "s_waitcnt vmcnt(17)\n STEP 144, 192, 160, 0\n OLD 240\n" \
  "s_waitcnt vmcnt(12)\n STEP 32, 208, 128, 1\n s_waitcnt lgkmcnt(8)\n OLD 16\n" \
  "s_waitcnt vmcnt(8)\n STEP 160, 224, 176, 2\n s_waitcnt lgkmcnt(8)\n OLD 48\n" \
  "s_waitcnt vmcnt(8)\n STEP 128, 240, 192, 3\n s_waitcnt lgkmcnt(8)\n" \
  "s_waitcnt vmcnt(4)\n STEP 176, 16, 0, 0\n s_waitcnt lgkmcnt(0)\n" \
  "s_waitcnt vmcnt(0)\n STEP 192, 48, 0, 1\n"
#define OPROJ_ASM \
  ".macro WDMA e\n" \
  "s_add_u32 m0, s49, (\\e)*4096\n s_xor_b32 s67, s66, %[rk]\n s_mul_i32 s67, s67, 40960\n" \
  "buffer_load_dwordx4 v0, s[36:39], s67 offen lds\n buffer_load_dwordx4 v1, s[36:39], s67 offen offset:1024 lds\n" \
  "buffer_load_dwordx4 v2, s[36:39], s67 offen offset:2048 lds\n buffer_load_dwordx4 v3, s[36:39], s67 offen offset:3072 lds\n" \
  "s_add_u32 s66, s66, 1\n s_and_b32 s66, s66, 15\n" \
  ".endm\n" \
  ".macro OLD ob\n" \
  "s_xor_b32 s67, s65, %[rk]\n s_lshl_b32 s67, s67, 10\n" \
  ".irp p, 0, 4, 8, 12\n buffer_load_dwordx4 v[\\ob+\\p:\\ob+\\p+3], v4, s[40:43], s67 offen sc1\n s_add_u32 s67, s67, 0x20000\n .endr\n" \
  "s_add_u32 s65, s65, 1\n s_and_b32 s65, s65, 15\n" \
  ".endm\n" \
  ".macro FLG\n buffer_load_dwordx2 v[6:7], v5, s[44:47], 0 offen sc1\n .endm\n" \
  ".macro SPIN i\n" \
  "1:\n FLG\n s_waitcnt vmcnt(0)\n" \
  "v_cmp_eq_u64 vcc, s[50:51], v[6:7]\n s_nop 0\n s_bitcmp1_b32 vcc_lo, \\i\n s_cbranch_scc1 2f\n" \
  "s_cmp_eq_u32 s68, 0\n s_cbranch_scc1 2f\n s_sub_u32 s68, s68, 1\n s_sleep 2\n s_branch 1b\n" \
  "2:\n" \
  ".endm\n" \
  ".macro CHK i, n\n" \
  "s_bitcmp1_b32 vcc_lo, \\i\n s_cbranch_scc1 2f\n s_waitcnt vmcnt(\\n)\n" \
  "v_cmp_eq_u64 vcc, s[50:51], v[6:7]\n s_nop 0\n s_bitcmp1_b32 vcc_lo, \\i\n s_cbranch_scc1 2f\n SPIN \\i\n" \
  ".endm\n" \
  ".macro WRD wd, e, n\n" \
  "ds_read_b64_tr_b16 v[\\wd+(\\n)*2:\\wd+(\\n)*2+1], v[8+(\\n)/2] offset:(\\e)*4096+((\\n)&1)*2048\n" \
  ".endm\n" \
  ".macro WRD8 wd, e\n .set nn, 0\n .rept 8\n WRD \\wd, \\e, nn\n .set nn, nn+1\n .endr\n .endm\n" \
  ".macro STEP wa, oa, wd, e\n .set nn, 0\n .rept 16\n" \
  "v_mfma_f32_16x16x32_bf16 v[64+nn*4:67+nn*4], v[\\wa+(nn/4)*4:\\wa+(nn/4)*4+3], v[\\oa+(nn&3)*4:\\oa+(nn&3)*4+3], v[64+nn*4:67+nn*4]\n" \
  ".if (\\wd > 0) && (nn < 8)\n WRD \\wd, \\e, nn\n .endif\n" \
  ".set nn, nn+1\n .endr\n .endm\n" \
  "s_mov_b32 s65, s48\n s_mov_b32 s66, s48\n s_mov_b32 s68, 0x40000\n" \
  "s_cmp_ge_u32 %[wv], 4\n s_cbranch_scc1 5f\n" \
  "WDMA 0\n WDMA 1\n WDMA 2\n WDMA 3\n" \
  "5:\n s_add_u32 s66, s48, 4\n s_and_b32 s66, s66, 15\n" \
  ".set nn, 64\n .rept 64\n v_mov_b32 v[nn], 0\n .set nn, nn+1\n .endr\n" \
  P2_BODY \
  "s_nop 15\n s_nop 7\n" \
  ".set nn, 0\n .rept 16\n ds_write_b128 v[12+nn/4], v[64+nn*4:67+nn*4] offset:(nn&3)*4096\n .set nn, nn+1\n .endr\n" \
  ".purgem WDMA\n .purgem OLD\n .purgem FLG\n .purgem SPIN\n .purgem CHK\n .purgem WRD\n .purgem WRD8\n .purgem STEP\n"

__global__ __launch_bounds__(512, 2) void fused_kernel(const char* xA, const char* kA, const char* vA, const char* wqA, const char* woA,
                                                       const char* xB, const char* kB, const char* vB, const char* wqB, const char* woB,
                                                       char* ws, char* out) {
  extern __shared__ __attribute__((aligned(16))) char smem[];
  const unsigned lds0 = (unsigned)(size_t)(__attribute__((address_space(3))) char*)smem;
  u64 pc; asm volatile("s_getpc_b64 %0" : "=s"(pc));
  const int tid = threadIdx.x, wave = __builtin_amdgcn_readfirstlane(tid >> 6);
  const int xcd = blockIdx.x & 7, r = blockIdx.x >> 3;
  const int br = xcd >> 2, head = (xcd & 3) * 8 + (r & 7), bp = r >> 3;
  const int mt = ((xcd >> 1) & 1) * 6 + r / 5, nt = (xcd & 1) * 5 + r % 5;
  const unsigned mine = lds0 + ((wave >> 1) + 3) % 4 * SLOT + (wave & 1) * 16384;
  const int rot = mt & 3, rk = (nt + r / 20 * 2 + ((xcd >> 1) & 1)) & 3;
  const unsigned wofs = wave * 512 * (DM * 2) + nt * 128;
  const char* wo = (br ? woB : woA) + wofs;
  u64* flags = (u64*)(ws + O_BYTES);
  u64* epoch = (u64*)(ws + O_BYTES + FLAG_BYTES + br * 128);
  if (tid >= 504) *LDSP(unsigned, lds0 + CTL + (tid - 504) * 4) = 0;
  if (__builtin_expect(wave >= 4, 1)) {
    const int lane = lane_id(), L = wave - 4, i8 = lane >> 3, c8 = lane & 7, i4 = lane >> 4, c16 = lane & 15;
    const unsigned st0 = (((r & 7) * 10) / 8 + (bp * 10) / 4) % 10;
    u32x4 vw, sw, sx, sc; u32x8 vx, vb, sb, sk;
#pragma unroll
    for (int p = 0; p < 4; ++p) vw[p] = (16 * L + 4 * p + i4) * (NH * DH * 2) - p * 1024 + ((c16 ^ (2 * (i4 | ((p >> 1) << 2)))) << 4);
    srd<0>(sw, (br ? wqB : wqA) + head * (DH * 2), DM * NH * DH * 2 - head * (DH * 2));
    sc[0] = lds0 + 24576 + L * 4096; sc[1] = lds0 + L * 6144;
    asm volatile("s_mov_b32 m0, %2\n s_nop 0\n .irp p, 0, 1, 2, 3\n buffer_load_dwordx4 v[12+\\p], s[40:43], %3 offen offset:1024*\\p lds\n .endr\n"
                 : "+{v[12:15]}"(vw), "+{s[40:43]}"(sw) : "s"(sc[0] + 3 * SLOT), "s"(st0 << 19) : "memory");
    __builtin_amdgcn_sched_barrier(0);
#pragma unroll
    for (int p = 0; p < 6; ++p) vx[2 + p] = (48 * L + 8 * p + i8) * (DM * 2) - (p % 3) * 1024 + ((c8 ^ ((4 * (p & 1) + (i8 >> 1)) & 7)) << 4);
    vx[0] = vx[1] = 0;
    srd<0>(sx, (br ? xB : xA) + (size_t)bp * 192 * (DM * 2), 192 * DM * 2);
    asm volatile("s_mov_b32 m0, %2\n s_nop 0\n .irp p, 0, 1, 2, 3, 4, 5\n buffer_load_dwordx4 v[6+\\p], s[36:39], %3 offen offset:1024*(\\p%%3) lds\n"
                 ".if \\p == 2\n s_add_u32 m0, m0, 3072\n s_nop 0\n .endif\n .endr\n"
                 : "+{v[4:11]}"(vx), "+{s[36:39]}"(sx) : "s"(sc[1] + 3 * SLOT), "s"(st0 << 7) : "memory", "scc");
    __builtin_amdgcn_sched_barrier(0);
    const size_t kvb = ((size_t)(2 * bp + (L >> 1)) * NH + head) * (SK * DH * 2) + (L & 1) * 4096;
#pragma unroll
    for (int p = 0; p < 4; ++p) vb[p] = (8 * p + i8) * (DM * 2) - p * 1024 + ((c8 ^ (2 * (i8 >> 1))) << 4);
    vb[4] = lds0 + (L >> 1) * 18432 + ((L & 1) * 16 + i4) * 288 + c16 * 16; vb[5] = lds0 + CTL + L; vb[6] = lds0 + CTL; vb[7] = 0;
    srd<0>(sk, (br ? kB : kA) + kvb, SK * DH * 2 - (L & 1) * 4096);
    srd<4>(sk, (br ? vB : vA) + kvb, SK * DH * 2 - (L & 1) * 4096);
    srd<0>(sb, wo, NH * DH * DM * 2 - wofs);
    sb[4] = st0 == 9 ? 0 : st0 + 1; sb[5] = 4 * rot + rk; sb[6] = mine; sb[7] = 24 + (L >> 1);
    sc[2] = r < 30 ? 36 : 32; sc[3] = (((r ^ bp) & 1) ? ORDER2 : ORDER) >> 1;
    u32x32 d0, d1, d2, d3, d4, d5; u32x16 e0; u32x8 e1;
    bar();
    asm volatile(LOADER_ASM
                 : "=&{v[64:95]}"(d0), "=&{v[96:127]}"(d1), "=&{v[128:159]}"(d2), "=&{v[160:191]}"(d3), "=&{v[192:223]}"(d4), "=&{v[224:255]}"(d5),
                   "=&{s[64:79]}"(e0), "=&{s[80:87]}"(e1)
                 : "{v0}"(lane * 16), "{v[4:11]}"(vx), "{v[12:15]}"(vw), "{v[16:23]}"(vb), "{s[36:39]}"(sx), "{s[40:43]}"(sw), "{s[44:51]}"(sk),
                   "{s[52:59]}"(sb), "{s[60:63]}"(sc), [kr] "s"((L >> 1) * 16 + (r >> 1) * 10 + xcd * 6)
                 : "memory", "scc", "vcc", "v24", "v25");
  } else {
    const int lane = lane_id(), i = lane & 15, g = lane >> 4, q4 = (lane >> 2) & 3, p4 = lane & 3;
    const unsigned ctl = lds0 + CTL, cme = ctl + 8 + wave;
    bar();
    if (wave == 0) *LDSP(volatile u64, ctl + 16) = __hip_atomic_load(epoch, __ATOMIC_RELAXED, __HIP_MEMORY_SCOPE_AGENT);
    u32x4 ad; u32x8 wa; u32x16 ls;
    ad[0] = lds0 + (wave >> 1) * 18432 + i * 288 + g * 16;
    ad[1] = lds0 + (wave >> 1) * 18432 + 9216 + (4 * g + q4) * 288 + p4 * 8;
    ad[2] = ctl + p4; ad[3] = cme;
#pragma unroll
    for (int c = 0; c < 8; ++c)
      wa[c] = lds0 + 3 * SLOT + 24576 + (8 * g + q4) * 256 + (p4 & 1) * 8 + (((2 * c + (p4 >> 1)) ^ (2 * (q4 | ((g & 1) << 2)))) << 4);
#pragma unroll
    for (int t = 0; t < 2; ++t) ls[t] = lds0 + 3 * SLOT + (48 * wave + i) * 128 + (((4 * t + g) ^ (i >> 1)) << 4);
    ls[2] = (r * 4 + wave) % 18 * 1024 + lane * 16;
    f32x4 acc[8][3];
    float lsum[3];
    {
      u32x32 o0, o1, o2, d0, d1, d2, d3; u32x16 e0;
      asm volatile(ATT_ASM
                   : "=&{v[160:191]}"(o0), "=&{v[192:223]}"(o1), "=&{v[224:255]}"(o2), "=&{v[32:63]}"(d0), "=&{v[64:95]}"(d1), "=&{v[96:127]}"(d2),
                     "=&{v[128:159]}"(d3), "=&{s[64:79]}"(e0), "+{v[0:3]}"(ad), "+{v[8:15]}"(wa), "+{v[16:31]}"(ls)
                   : [scl] "n"(__builtin_bit_cast(unsigned, SCL)), [pc] "s"(pc)
                   : "memory", "scc", "vcc", "v4", "v5", "v6", "v7");
#pragma unroll
      for (int k = 0; k < 96; ++k) acc[k / 12][(k / 4) % 3][k & 3] = __uint_as_float(k < 32 ? o0[k & 31] : k < 64 ? o1[k & 31] : o2[k & 31]);
#pragma unroll
      for (int b = 0; b < 3; ++b) lsum[b] = __uint_as_float(ls[4 + 4 * b]);
    }
    post(ad[3], 27);
    const int ln = lane_id();
    const __amdgpu_buffer_rsrc_t ro = __builtin_amdgcn_make_buffer_rsrc(ws, 0, O_BYTES, 0x00020000);
    const unsigned oo = ((br * 48 + bp * 12 + 3 * wave) * 128 + head * 4) * 1024;
#pragma unroll
    for (int b = 0; b < 3; ++b) {
      const float inv = 1.0f / lsum[b];
#pragma unroll
      for (int s = 0; s < 4; ++s)
        __builtin_amdgcn_raw_buffer_store_b128(__builtin_bit_cast(u32x4, pk8(acc[2 * s][b] * inv, acc[2 * s + 1][b] * inv)), ro,
                                               oo + b * 131072 + ln * 16 + s * 1024, 0, 16);
    }
    asm volatile("s_waitcnt vmcnt(0)" ::: "memory");
    if (ln == 0 && __hip_atomic_fetch_add(LDSP(unsigned, ctl + 24), 1u, __ATOMIC_RELAXED, __HIP_MEMORY_SCOPE_WORKGROUP) == 3)
      __hip_atomic_store(flags + ((br * 4 + bp) * 32 + head) * 16, *LDSP(volatile u64, ctl + 16) + TAG, __ATOMIC_RELAXED, __HIP_MEMORY_SCOPE_AGENT);
    await(ctl + 8, 0, 27);
  }
  if (r >= 30) {
    if (r == 31 && (xcd & 3) == 0 && wave == 4) {
      const int lane = lane_id();
      const u64 ep = *LDSP(volatile u64, lds0 + CTL + 16);
      const u64* f = flags + (br * 128 + lane) * 16;
      for (int k = 0; k < (1 << 18); ++k) {
        const u64 a = __hip_atomic_load(f, __ATOMIC_RELAXED, __HIP_MEMORY_SCOPE_AGENT), b = __hip_atomic_load(f + 64 * 16, __ATOMIC_RELAXED, __HIP_MEMORY_SCOPE_AGENT);
        if (__all(a == ep + TAG && b == ep + TAG)) break;
        __builtin_amdgcn_s_sleep(16);
      }
      if (lane == 0) __hip_atomic_store(epoch, ep + 1, __ATOMIC_RELAXED, __HIP_MEMORY_SCOPE_AGENT);
    }
    return;
  }
  {
    const u64 ep = *LDSP(volatile u64, lds0 + CTL + 16);
    const int lane = lane_id(), i = lane & 15, g = lane >> 4, q4 = (lane >> 2) & 3, p4 = lane & 3, i8 = lane >> 3, c8 = lane & 7;
    u32x16 va, sa;
#pragma unroll
    for (int p = 0; p < 4; ++p) {
      va[p] = (8 * p + i8) * (DM * 2) - p * 1024 + ((c8 ^ (2 * (i8 >> 1))) << 4);
      va[8 + p] = mine + (4 * g + q4) * 128 + (p4 & 1) * 8 + (((2 * p + (p4 >> 1)) ^ (2 * (2 * (g & 1) + (q4 >> 1)))) << 4);
      va[12 + p] = mine + i * 256 + (((4 * p + g) ^ i) << 4);
    }
    va[4] = lane * 16; va[5] = lane < 4 ? ((rot + lane) & 3) * 128 : 512; va[6] = va[7] = va[4];
    const unsigned oofs = ((br * 48 + mt * 4) * 128 + wave * 16) * 1024;
    srd<0>(sa, wo, NH * DH * DM * 2 - wofs);
    srd<4>(sa, ws + oofs, O_BYTES - oofs);
    srd<8>(sa, (const char*)(flags + ((br * 4 + mt / 3) * 32 + wave * 4) * 16), 512);
    const u64 want = ep + TAG;
    sa[12] = 4 * rot; sa[13] = mine;
    sa[14] = __builtin_amdgcn_readfirstlane((unsigned)want); sa[15] = __builtin_amdgcn_readfirstlane((unsigned)(want >> 32));
    u32x32 d0, d1, d2, d3, d4, d5, d6; u32x16 d7; u32x8 e0;
    asm volatile(OPROJ_ASM
                 : "=&{v[64:95]}"(d0), "=&{v[96:127]}"(d1), "=&{v[128:159]}"(d2), "=&{v[160:191]}"(d3), "=&{v[192:223]}"(d4), "=&{v[224:255]}"(d5),
                   "=&{v[32:63]}"(d6), "=&{v[16:31]}"(d7), "=&{s[64:71]}"(e0), "+{v[0:15]}"(va)
                 : "{s[36:51]}"(sa), [wv] "s"(wave), [rk] "s"(rk)
                 : "memory", "scc", "vcc");
    bar();
    const int l2 = lane_id(), c2 = l2 & 7, row = wave * 8 + (l2 >> 3);
    const unsigned a0 = lds0 + row * 256 + (((2 * c2) ^ (row & 15)) << 4), a1 = a0 ^ 16;
    f32x4 lo = *LDSP(const f32x4, a0), hi = *LDSP(const f32x4, a1);
#pragma unroll
    for (int w = 1; w < 8; ++w) {
      const unsigned o = (w >> 1) * SLOT + (w & 1) * 16384;
      lo += *LDSP(const f32x4, a0 + o); hi += *LDSP(const f32x4, a1 + o);
    }
    __builtin_amdgcn_raw_buffer_store_b128(__builtin_bit_cast(u32x4, pk8(lo, hi)), __builtin_amdgcn_make_buffer_rsrc(out, 0, 768 * 2 * DM * 2, 0x00020000),
                                           (mt * 64 + row) * (2 * DM * 2) + br * (DM * 2) + nt * 128 + c2 * 16, 0, 0);
  }
}

extern "C" void kernel_launch(void* const* d_in, const int* in_sizes, int n_in, void* d_out, int out_size, void* d_ws, size_t ws_size,
                              hipStream_t stream) {
  (void)in_sizes; (void)n_in; (void)out_size; (void)ws_size;
  (void)hipFuncSetAttribute((const void*)fused_kernel, hipFuncAttributeMaxDynamicSharedMemorySize, LDS_BYTES);
  fused_kernel<<<256, 512, LDS_BYTES, stream>>>((const char*)d_in[0], (const char*)d_in[1], (const char*)d_in[2], (const char*)d_in[3], (const char*)d_in[4],
                                                (const char*)d_in[5], (const char*)d_in[6], (const char*)d_in[7], (const char*)d_in[8], (const char*)d_in[9],
                                                (char*)d_ws, (char*)d_out);
}
```
